```python
import torch

def _softmax(x, axis=-1):
    return torch.softmax(x, dim=axis)

def _gelu(x):
    return torch.nn.functional.gelu(x, approximate='tanh')

def _silu(x):
    return torch.nn.functional.silu(x)

def _sigmoid(x):
    return torch.sigmoid(x)

def _layernorm(x, axis=-1, eps=1e-05):
    m = x.mean(dim=axis, keepdim=True)
    v = x.var(dim=axis, keepdim=True, unbiased=False)
    return (x - m) / torch.sqrt(v + eps)

def _scatter_add(tgt, idx, src, axis=0):
    return tgt.clone().index_add_(axis, idx, src)

def _segment_sum(src, seg, n):
    out = torch.zeros((n,) + src.shape[1:], dtype=src.dtype)
    return out.index_add_(0, seg, src)

def _pad(x, pw):
    flat = tuple((v for pair in reversed(pw) for v in pair))
    return torch.nn.functional.pad(x, flat)
kernel_name = 'sequential_preLN__memory_aug_recurrent_mem_tr__glu_gated_bilinear__prompt'

def setup_inputs():

    _w = lambda a: (lambda t: t.to(torch.bfloat16) if t.is_floating_point() else t)(torch.as_tensor(a))

    def _A_setup():
        B, S, Dm = (1, 2048, 640)
        H, Dh = (32, 192)
        M, seg = (8, 32)
        seg = min(seg, S)
        S = S // seg * seg
        return {'x': _w((lambda *s: torch.randn(*s, dtype=torch.bfloat16) * 0.05)(B, S, Dm)), 'mem0': _w((lambda *s: torch.randn(*s, dtype=torch.bfloat16) * 0.2)(B, M, Dm)), 'Wq': _w((lambda *s: torch.randn(*s, dtype=torch.bfloat16) * 1.6)(Dm, H, Dh) * Dm ** (-0.5)), 'Wk': _w((lambda *s: torch.randn(*s, dtype=torch.bfloat16) * 1.6)(Dm, H, Dh) * Dm ** (-0.5)), 'Wv': _w((lambda *s: torch.randn(*s, dtype=torch.bfloat16) * 0.2)(Dm, H, Dh) * Dm ** (-0.5)), 'Wo': _w((lambda *s: torch.randn(*s, dtype=torch.bfloat16) * 0.2)(H, Dh, Dm) * (H * Dh) ** (-0.5)), 'seg_len': seg}
    _A_in = _A_setup()

    def _B_setup():
        B, S, D, F = (1, 2048, 640, 1536)
        return {'x': _w((lambda *s: torch.randn(*s, dtype=torch.bfloat16) * 0.05)(B, S, D)), 'w_gate': _w((lambda *s: torch.randn(*s, dtype=torch.bfloat16) * 0.2)(D, F) * D ** (-0.5)), 'w_up': _w((lambda *s: torch.randn(*s, dtype=torch.bfloat16) * 0.2)(D, F) * D ** (-0.5)), 'w_down': _w((lambda *s: torch.randn(*s, dtype=torch.bfloat16) * 0.2)(F, D) * F ** (-0.5))}
    _B_in = _B_setup()
    return {'A_x': _A_in['x'], 'A_mem0': _A_in['mem0'], 'A_Wq': _A_in['Wq'], 'A_Wk': _A_in['Wk'], 'A_Wv': _A_in['Wv'], 'A_Wo': _A_in['Wo'], 'A_seg_len': _A_in['seg_len'], 'B_x': _B_in['x'], 'B_w_gate': _B_in['w_gate'], 'B_w_up': _B_in['w_up'], 'B_w_down': _B_in['w_down']}

def reference(A_x, A_mem0, A_Wq, A_Wk, A_Wv, A_Wo, A_seg_len, B_x, B_w_gate, B_w_up, B_w_down):

    def _A_ref(x, mem0, Wq, Wk, Wv, Wo, seg_len):
        B, S, Dm = x.shape
        H, Dh = (Wq.shape[1], Wq.shape[2])
        M = mem0.shape[1]
        scale = Dh ** (-0.5)
        n_seg = S // seg_len
        L = M + seg_len
        idx = torch.arange(L)
        mask = (idx[:, None] < M) | (idx[:, None] >= idx[None, :])
        mem = mem0
        outs = []
        for s in range(n_seg):
            xs = x[:, s * seg_len:(s + 1) * seg_len, :].clone()
            h = torch.concatenate([mem, xs], axis=1)
            q = torch.einsum('btd,dhe->bhte', h, Wq)
            k = torch.einsum('btd,dhe->bhte', h, Wk)
            v = torch.einsum('btd,dhe->bhte', h, Wv)
            scores = torch.einsum('bhqe,bhke->bhqk', q, k) * scale
            scores = torch.where(mask[None, None], scores, -1000000000.0)
            p = _softmax(scores, axis=-1)
            o = torch.einsum('bhqk,bhke->bhqe', p, v)
            o = torch.einsum('bhte,hed->btd', o, Wo)
            mem = o[:, :M, :].clone()
            outs.append(o[:, M:, :])
        return torch.concatenate(outs, axis=1)

    def _B_ref(x, w_gate, w_up, w_down):
        g = torch.einsum('bsd,df->bsf', x, w_gate)
        u = torch.einsum('bsd,df->bsf', x, w_up)
        h = g * u
        return torch.einsum('bsf,fd->bsd', h, w_down)
    x = A_x
    h = x + _A_ref(_layernorm(x, axis=-1), A_mem0, A_Wq, A_Wk, A_Wv, A_Wo, A_seg_len)
    h2 = h + _B_ref(_layernorm(h, axis=-1), B_w_gate, B_w_up, B_w_down)
    return h2
```

```cpp
#include <hip/hip_runtime.h>
#include <hip/hip_bf16.h>
#include <stdint.h>

typedef unsigned short bf16_t;
using bf16x8 = __attribute__((ext_vector_type(8))) short;
using bf16x4 = __attribute__((ext_vector_type(4))) short;
using f32x4 = __attribute__((ext_vector_type(4))) float;
typedef unsigned v4u __attribute__((ext_vector_type(4)));
typedef unsigned v2u __attribute__((ext_vector_type(2)));

constexpr int S_ = 2048, DM = 640, NH = 32, DH = 192, HD = NH * DH;
constexpr int MM = 8, SEG = 32, NSEG = S_ / SEG;
constexpr int FF = 1536;
constexpr int KZ = NH * (MM + SEG);
constexpr int NCONS = 80;

constexpr size_t OFF_XN = 0;
constexpr size_t OFF_WQKT = OFF_XN + (size_t)S_ * DM * 2;
constexpr size_t OFF_WVT = OFF_WQKT + (size_t)2 * HD * DM * 2;
constexpr size_t OFF_WOT = OFF_WVT + (size_t)HD * DM * 2;
constexpr size_t OFF_WGUT = OFF_WOT + (size_t)HD * DM * 2;
constexpr size_t OFF_WDT = OFF_WGUT + (size_t)2 * FF * DM * 2;
constexpr size_t OFF_QK = OFF_WDT + (size_t)FF * DM * 2;
constexpr size_t OFF_DT = OFF_QK + (size_t)S_ * 2 * HD * 2;
constexpr size_t OFF_CF = OFF_DT + (size_t)NSEG * DM * NH * SEG * 2;
constexpr size_t OFF_UT = OFF_CF + (size_t)NH * DM * DM * 2;
constexpr size_t OFF_KM = OFF_UT + (size_t)NSEG * DM * NH * MM * 2;
constexpr size_t OFF_PX = OFF_KM + (size_t)NSEG * MM * HD * 2;
constexpr size_t OFF_H = OFF_PX + (size_t)S_ * KZ * 2;
constexpr size_t OFF_HN = OFF_H + (size_t)S_ * DM * 2;
constexpr size_t OFF_GU = OFF_HN + (size_t)S_ * DM * 2;
constexpr size_t OFF_MS = OFF_GU + (size_t)S_ * FF * 2;
constexpr size_t MS_BYTES = (size_t)MM * DM * 2;
constexpr size_t OFF_PS = OFF_MS + (size_t)(NSEG + 1) * MS_BYTES;
constexpr size_t PS_BYTES = (size_t)MM * KZ * 2;
constexpr size_t OFF_ERR = OFF_PS + (size_t)NSEG * PS_BYTES;
constexpr size_t OFF_FL = OFF_ERR + 256;
constexpr int NRB = 8;
constexpr int NFB = 16;
#define FIN 8
constexpr int FL_K = 0, FL_D = NRB * NH, FL_Q = FL_D + NRB, FL_KM = FL_Q + NRB, FL_U = FL_KM + NSEG, FL_PX = FL_U + NSEG,
              FL_H = FL_PX + NSEG, FL_LN = FL_H + NSEG, FL_G5 = FL_LN + NFB, FL_K0 = FL_G5 + NFB, FL_D0 = FL_K0 + 4 * NH,
              FL_N = FL_D0 + 4;
constexpr size_t WS_END = OFF_FL + (size_t)FL_N * 128;
static_assert(WS_END <= (size_t)256 * 1024 * 1024, "workspace");

__device__ __forceinline__ float bf2f(unsigned short b) { return __uint_as_float(((unsigned)b) << 16); }
__device__ __forceinline__ unsigned short f2bf(float f) {
    __hip_bfloat16 h = __float2bfloat16(f);
    return *reinterpret_cast<unsigned short*>(&h);
}
__device__ __forceinline__ float rbf(float f) { return bf2f(f2bf(f)); }
__device__ __forceinline__ unsigned pack2(float a, float b) {
    typedef __bf16 bf2 __attribute__((ext_vector_type(2)));
    typedef float f2 __attribute__((ext_vector_type(2)));
    f2 v = {a, b};
    return __builtin_bit_cast(unsigned, __builtin_convertvector(v, bf2));
}
__device__ __forceinline__ unsigned sanit(unsigned u) { return u == 0xFFFFFFFFu ? 0x7FC07FC0u : u; }
template <int CTRL> __device__ __forceinline__ float dppf(float x) {
    return __builtin_bit_cast(float, __builtin_amdgcn_update_dpp(0, __builtin_bit_cast(int, x), CTRL, 0xf, 0xf, true));
}
template <int CTRL> __device__ __forceinline__ unsigned dppu(unsigned x) {
    return (unsigned)__builtin_amdgcn_update_dpp(0, (int)x, CTRL, 0xf, 0xf, true);
}

__device__ __forceinline__ void ln_row_regs(const bf16_t* __restrict__ src, int lane, unsigned (&o)[5]) {
    float v[10];
    const unsigned* s32 = reinterpret_cast<const unsigned*>(src);
#pragma unroll
    for (int i = 0; i < 5; ++i) { unsigned u = s32[lane + 64 * i]; v[2 * i] = bf2f(u & 0xffff); v[2 * i + 1] = bf2f(u >> 16); }
    float sum = 0.f;
#pragma unroll
    for (int i = 0; i < 10; ++i) sum += v[i];
#pragma unroll
    for (int o = 32; o >= 1; o >>= 1) sum += __shfl_xor(sum, o);
    const float mean = sum * (1.0f / DM);
    float sq = 0.f;
#pragma unroll
    for (int i = 0; i < 10; ++i) { float d = v[i] - mean; sq += d * d; }
#pragma unroll
    for (int o = 32; o >= 1; o >>= 1) sq += __shfl_xor(sq, o);
    const float mb = rbf(mean);
    const float vb = rbf(sq * (1.0f / DM));
    const float den = rbf(sqrtf(rbf(vb + 1e-5f)));
#pragma unroll
    for (int i = 0; i < 5; ++i) {
        float a = rbf(v[2 * i] - mb) / den, b = rbf(v[2 * i + 1] - mb) / den;
        o[i] = pack2(a, b);
    }
}
__device__ __forceinline__ void ln_row(const bf16_t* __restrict__ src, bf16_t* __restrict__ dst, int lane) {
    unsigned o[5];
    ln_row_regs(src, lane, o);
    unsigned* d32 = reinterpret_cast<unsigned*>(dst);
#pragma unroll
    for (int i = 0; i < 5; ++i) d32[lane + 64 * i] = o[i];
}

__device__ __forceinline__ void init_slots(const bf16_t* __restrict__ mem0, unsigned char* __restrict__ ws, int bid, int nb) {
    const size_t n16 = ((size_t)NSEG * MS_BYTES + (size_t)NSEG * PS_BYTES) / 16;
    v4u* fill = reinterpret_cast<v4u*>(ws + OFF_MS + MS_BYTES);
    const v4u ones = {0xFFFFFFFFu, 0xFFFFFFFFu, 0xFFFFFFFFu, 0xFFFFFFFFu};
    for (size_t i = bid * 256 + threadIdx.x; i < n16; i += (size_t)nb * 256) fill[i] = ones;
    if (bid == 0) {
        for (int i = threadIdx.x; i < 640; i += 256) {
            int c = i >> 3, tok = i & 7;
            v4u v = *reinterpret_cast<const v4u*>(mem0 + tok * DM + c * 8);
            v.x = sanit(v.x); v.y = sanit(v.y); v.z = sanit(v.z); v.w = sanit(v.w);
            reinterpret_cast<v4u*>(ws + OFF_MS)[i] = v;
        }
        if (threadIdx.x < 64) reinterpret_cast<unsigned*>(ws + OFF_ERR)[threadIdx.x] = 0;
        for (int i = threadIdx.x; i < FL_N * 32; i += 256) reinterpret_cast<unsigned*>(ws + OFF_FL)[i] = 0;
    }
}

template <int ROWS>
__device__ __forceinline__ void stage_tile(const bf16_t* __restrict__ g, int ld, int kt, unsigned char* lds, int tid) {
    constexpr int NI = ROWS / 32;
#pragma unroll
    for (int i = 0; i < NI; ++i) {
        const int b = tid + i * 256, row = b >> 3, ch = (b & 7) ^ (row & 7);
        __builtin_amdgcn_global_load_lds((const unsigned*)(g + (size_t)row * ld + kt * 64 + ch * 8),
                                         (unsigned*)(lds + (size_t)(i * 256 + (tid & ~63)) * 16), 16, 0, 0);
    }
}
__device__ __forceinline__ bf16x8 lds_frag(const unsigned char* lds, int row, int ch) {
    return *reinterpret_cast<const bf16x8*>(lds + row * 128 + ((ch ^ (row & 7)) << 4));
}

template <int COLS, bool PAIR = false>
__device__ __forceinline__ void stage_kn(const bf16_t* g, int ld, int kt, unsigned char* lds, int tid, long d2 = 0) {
    constexpr int NSUB = COLS / 32;
#pragma unroll
    for (int i = 0; i < NSUB; ++i) {
        const int L = tid + i * 256, band = L / (NSUB * 32), sub = (L % (NSUB * 32)) >> 5, row = band * 8 + ((L >> 2) & 7);
        const int col = (sub * 4 + ((L & 3) ^ ((row >> 2) & 3))) * 8;
        const long co = (PAIR && col >= COLS / 2) ? d2 + (col - COLS / 2) : (long)col;
        __builtin_amdgcn_global_load_lds((const unsigned*)(g + co + (long)(kt * 64 + row) * ld),
                                         (unsigned*)(lds + (size_t)(i * 256 + (tid & ~63)) * 16), 16, 0, 0);
    }
}
typedef short s16x4 __attribute__((ext_vector_type(4)));
template <int COLS>
__device__ __forceinline__ bf16x8 frag_kn(const unsigned char* lds, int c16, int ks, int lane) {
    constexpr int NSUB = COLS / 32;
    const int g = lane >> 4, q = (lane & 15) >> 2, p = lane & 3, ch = 2 * c16 + (p >> 1);
    s16x4 r[2];
#pragma unroll
    for (int t = 0; t < 2; ++t) {
        const int row = 32 * ks + 8 * g + 4 * t + q;
        const int off = (row >> 3) * (NSUB * 512) + (ch >> 2) * 512 + (row & 7) * 64 + 16 * ((ch & 3) ^ ((row >> 2) & 3)) + 8 * (p & 1);
        r[t] = __builtin_amdgcn_ds_read_tr16_b64_v4i16((__attribute__((address_space(3))) s16x4*)(lds + off));
    }
    return __builtin_shufflevector(r[0], r[1], 0, 1, 2, 3, 4, 5, 6, 7);
}

template <int BM, int BN, bool SWAP, int NS, bool GU = false>
__device__ __forceinline__ void gemm_mainloop_ms(const bf16_t* __restrict__ A, int lda, const bf16_t* __restrict__ B, int ldb,
                                                 int K, unsigned char* lds, f32x4 (&acc)[BM / 32][BN / 32],
                                                 long d2 = 0) {
    constexpr int TM = BM / 32, TN = BN / 32;
    constexpr int ABYTES = BM * 128, BBYTES = BN * 128, BUF = ABYTES + BBYTES;
    constexpr int LPT = BM / 32 + BN / 32;
    static_assert((NS - 2) * LPT <= 63, "vmcnt");
    int tid = threadIdx.x;
    asm volatile("" : "+v"(tid));
    const int lane = tid & 63, wid = tid >> 6, wr = wid >> 1, wc = wid & 1;
    const int fr = lane & 15, fq = lane >> 4;
    const int nt = K / 64;
#pragma unroll
    for (int p = 0; p < NS - 1; ++p) {
        stage_tile<BM>(A, lda, p, lds + p * BUF, tid);
        stage_kn<BN, GU>(B, ldb, p, lds + p * BUF + ABYTES, tid, d2);
    }
    int cb = 0;
    for (int t = 0; t < nt; ++t) {
        if (t + NS - 1 <= nt) asm volatile("s_waitcnt vmcnt(%0)" ::"n"((NS - 2) * LPT) : "memory");
        else asm volatile("s_waitcnt vmcnt(0)" ::: "memory");
        asm volatile("s_waitcnt lgkmcnt(0)\n\ts_barrier" ::: "memory");
        if (t + NS - 1 < nt) {
            const int nb = cb == 0 ? NS - 1 : cb - 1;
            stage_tile<BM>(A, lda, t + NS - 1, lds + nb * BUF, tid);
            stage_kn<BN, GU>(B, ldb, t + NS - 1, lds + nb * BUF + ABYTES, tid, d2);
        }
        const unsigned char* cur = lds + cb * BUF;
#pragma unroll
        for (int kk = 0; kk < 2; ++kk) {
            bf16x8 af[TM], bfr[TN];
#pragma unroll
            for (int m = 0; m < TM; ++m) af[m] = lds_frag(cur, wr * (BM / 2) + m * 16 + fr, kk * 4 + fq);
#pragma unroll
            for (int n = 0; n < TN; ++n)
                bfr[n] = frag_kn<BN>(cur + ABYTES, GU ? (n & 1) * (BN / 32) + wc * (TN / 2) + (n >> 1) : wc * TN + n, kk, lane);
#pragma unroll
            for (int m = 0; m < TM; ++m)
#pragma unroll
                for (int n = 0; n < TN; ++n)
                    acc[m][n] = SWAP ? __builtin_amdgcn_mfma_f32_16x16x32_bf16(bfr[n], af[m], acc[m][n], 0, 0, 0)
                                     : __builtin_amdgcn_mfma_f32_16x16x32_bf16(af[m], bfr[n], acc[m][n], 0, 0, 0);
        }
        cb = cb == NS - 1 ? 0 : cb + 1;
    }
    __syncthreads();
}

__device__ __forceinline__ void body_g3(const bf16_t* __restrict__ Wv, const bf16_t* __restrict__ Wo, bf16_t* __restrict__ Cf,
                                        int bid, unsigned char* lds) {
    const int h = bid / 5, bn = bid % 5;
    const int tid = threadIdx.x, lane = tid & 63, wid = tid >> 6, wr = wid >> 1, wc = wid & 1, fr = lane & 15, fq = lane >> 4;
    unsigned char* bimg = lds;
    unsigned char* abuf = lds + 48 * 1024;
#pragma unroll
    for (int kt = 0; kt < 3; ++kt) stage_kn<128>(Wo + (size_t)h * DH * DM + bn * 128, DM, kt, bimg + kt * 16384, tid);
#pragma unroll
    for (int kt = 0; kt < 3; ++kt) stage_tile<128>(Wv + h * DH, HD, kt, abuf + kt * 16384, tid);
    for (int bm = 0; bm < 5; ++bm) {
        asm volatile("s_waitcnt vmcnt(0)" ::: "memory");
        __syncthreads();
        const unsigned char* cur = abuf + (bm & 1) * 49152;
        if (bm + 1 < 5) {
#pragma unroll
            for (int kt = 0; kt < 3; ++kt)
                stage_tile<128>(Wv + (size_t)(bm + 1) * 128 * HD + h * DH, HD, kt, abuf + ((bm + 1) & 1) * 49152 + kt * 16384, tid);
        }
        f32x4 acc[4][4] = {};
#pragma unroll
        for (int kt = 0; kt < 3; ++kt)
#pragma unroll
            for (int kk = 0; kk < 2; ++kk) {
                bf16x8 af[4], bfr[4];
#pragma unroll
                for (int m = 0; m < 4; ++m) af[m] = lds_frag(cur + kt * 16384, wr * 64 + m * 16 + fr, kk * 4 + fq);
#pragma unroll
                for (int n = 0; n < 4; ++n) bfr[n] = frag_kn<128>(bimg + kt * 16384, wc * 4 + n, kk, lane);
#pragma unroll
                for (int m = 0; m < 4; ++m)
#pragma unroll
                    for (int n = 0; n < 4; ++n) acc[m][n] = __builtin_amdgcn_mfma_f32_16x16x32_bf16(af[m], bfr[n], acc[m][n], 0, 0, 0);
            }
#pragma unroll
        for (int m = 0; m < 4; ++m)
#pragma unroll
            for (int n = 0; n < 4; ++n) {
                const int d = bm * 128 + wr * 64 + m * 16 + fq * 4;
                const int col = bn * 128 + wc * 64 + n * 16 + fr;
                const int c = col >> 3, cc = col & 7, tau = h >> 1, w = tau >> 2, t = tau & 3;
                const int ln = ((h & 1) * 8 + cc) + 16 * ((d >> 3) & 3), ms = d >> 5;
                v2u o; o.x = pack2(acc[m][n][0], acc[m][n][1]); o.y = pack2(acc[m][n][2], acc[m][n][3]);
                *reinterpret_cast<v2u*>(Cf + ((((size_t)(c * 4 + w) * 4 + t) * 20 + ms) * 64 + ln) * 8 + (d & 7)) = o;
            }
    }
}

__global__ __launch_bounds__(256) void k_pre(const bf16_t* __restrict__ x, const bf16_t* __restrict__ mem0, const bf16_t* __restrict__ Wv,
                                             const bf16_t* __restrict__ Wo, unsigned char* __restrict__ ws) {
    extern __shared__ __attribute__((aligned(16))) unsigned char lds[];
    const int b = blockIdx.x;
    if (b < 64) {
#pragma unroll 4
        for (int r = 0; r < 8; ++r) {
            const int row = b * 32 + r * 4 + (threadIdx.x >> 6);
            ln_row(x + (size_t)row * DM, reinterpret_cast<bf16_t*>(ws + OFF_XN) + (size_t)row * DM, threadIdx.x & 63);
        }
    } else if (b < 96) init_slots(mem0, ws, b - 64, 32);
    else body_g3(Wv, Wo, (bf16_t*)(ws + OFF_CF), b - 96, lds);
}

typedef __attribute__((address_space(1))) unsigned gu32;
template <int BM>
__device__ __forceinline__ void tile_k(const bf16_t* __restrict__ XN, const bf16_t* __restrict__ Wcols, __amdgpu_buffer_rsrc_t rqk,
                                       int sb, int ncol0, unsigned char* lds) {
    constexpr int BN = 192;
    f32x4 acc[BM / 32][BN / 32] = {};
    gemm_mainloop_ms<BM, BN, true, BM == 64 ? 4 : 3>(XN + (size_t)sb * BM * DM, DM, Wcols, HD, DM, lds, acc);
    int tid = threadIdx.x;
    asm volatile("" : "+v"(tid));
    const int lane = tid & 63, wid = tid >> 6, wr = wid >> 1, wc = wid & 1, fr = lane & 15, fq = lane >> 4;
#pragma unroll
    for (int m = 0; m < BM / 32; ++m)
#pragma unroll
        for (int n = 0; n < BN / 32; ++n) {
            const int tok = sb * BM + wr * (BM / 2) + m * 16 + fr, col = ncol0 + wc * (BN / 2) + n * 16 + fq * 4;
            v2u o; o.x = pack2(acc[m][n][0], acc[m][n][1]); o.y = pack2(acc[m][n][2], acc[m][n][3]);
            __builtin_amdgcn_raw_buffer_store_b64(o, rqk, (tok * (2 * HD) + col) * 2, 0, 16);
        }
}
template <int BM>
__device__ __forceinline__ void tile_vd(const bf16_t* __restrict__ XN, const bf16_t* __restrict__ Wv, const bf16_t* __restrict__ Wo,
                                        __amdgpu_buffer_rsrc_t rdt, int sb, int h, unsigned char* lds) {
    constexpr int BN = 192, TM = BM / 32;
    unsigned char* vimg = lds + 96 * 1024;
    {
        f32x4 acc[BM / 32][BN / 32] = {};
        gemm_mainloop_ms<BM, BN, false, 3>(XN + (size_t)sb * BM * DM, DM, Wv + h * DH, HD, DM, lds, acc);
        int tid = threadIdx.x;
        asm volatile("" : "+v"(tid));
        const int lane = tid & 63, wid = tid >> 6, wr = wid >> 1, wc = wid & 1, fr = lane & 15, fq = lane >> 4;
#pragma unroll
        for (int kt = 0; kt < 3; ++kt) stage_kn<128>(Wo + (size_t)h * DH * DM, DM, kt, lds + kt * 16384, tid);
#pragma unroll
        for (int m = 0; m < BM / 32; ++m)
#pragma unroll
            for (int n = 0; n < BN / 32; ++n)
#pragma unroll
                for (int j = 0; j < 4; ++j) {
                    const int row = wr * (BM / 2) + m * 16 + fq * 4 + j, e = wc * (BN / 2) + n * 16 + fr;
                    const int kt = e >> 6, ch = (e & 63) >> 3;
                    *reinterpret_cast<bf16_t*>(vimg + kt * (BM * 128) + row * 128 + ((ch ^ (row & 7)) << 4) + (e & 7) * 2) = f2bf(acc[m][n][j]);
                }
    }
#pragma unroll 1
    for (int cb = 0; cb < DM / 128; ++cb) {
        int tid = threadIdx.x;
        asm volatile("" : "+v"(tid));
        const int lane = tid & 63, wid = tid >> 6, wr = wid >> 1, wc = wid & 1, fr = lane & 15, fq = lane >> 4;
        asm volatile("s_waitcnt vmcnt(0)" ::: "memory");
        __syncthreads();
        const unsigned char* cur = lds + (cb & 1) * 49152;
        if (cb + 1 < DM / 128) {
#pragma unroll
            for (int kt = 0; kt < 3; ++kt)
                stage_kn<128>(Wo + (size_t)h * DH * DM + (cb + 1) * 128, DM, kt, lds + ((cb + 1) & 1) * 49152 + kt * 16384, tid);
        }
        f32x4 acc[TM][4] = {};
#pragma unroll
        for (int kt = 0; kt < 3; ++kt)
#pragma unroll
            for (int kk = 0; kk < 2; ++kk) {
                bf16x8 af[TM], bfr[4];
#pragma unroll
                for (int m = 0; m < TM; ++m) af[m] = lds_frag(vimg + kt * (BM * 128), wr * (BM / 2) + m * 16 + fr, kk * 4 + fq);
#pragma unroll
                for (int n = 0; n < 4; ++n) bfr[n] = frag_kn<128>(cur + kt * 16384, wc * 4 + n, kk, lane);
#pragma unroll
                for (int m = 0; m < TM; ++m)
#pragma unroll
                    for (int n = 0; n < 4; ++n) acc[m][n] = __builtin_amdgcn_mfma_f32_16x16x32_bf16(af[m], bfr[n], acc[m][n], 0, 0, 0);
            }
#pragma unroll
        for (int m = 0; m < TM; ++m)
#pragma unroll
            for (int n = 0; n < 4; ++n) {
                const int row = sb * BM + wr * (BM / 2) + m * 16 + fq * 4;
                const int col = cb * 128 + wc * 64 + n * 16 + fr;
                const int s = row >> 5, jx = row & 31;
                v2u o; o.x = pack2(acc[m][n][0], acc[m][n][1]); o.y = pack2(acc[m][n][2], acc[m][n][3]);
                __builtin_amdgcn_raw_buffer_store_b64(o, rdt, ((((s * NCONS + (col >> 3)) * NH + h) * 8 + (col & 7)) * 32 + jx) * 2, 0, 16);
            }
    }
}
__device__ __forceinline__ void publish_add(unsigned char* ws, int flag) {
    asm volatile("s_waitcnt vmcnt(0)" ::: "memory");
    __syncthreads();
    if (threadIdx.x == 0)
        __hip_atomic_fetch_add((gu32*)(ws + OFF_FL + (size_t)flag * 128), 1u, __ATOMIC_RELAXED, __HIP_MEMORY_SCOPE_AGENT);
}
__device__ __forceinline__ bool wait_flag(unsigned char* ws, int flag, unsigned need) {
    gu32* f = (gu32*)(ws + OFF_FL + (size_t)flag * 128);
    for (int spins = 0; spins < (1 << 22); ++spins) {
        if (__hip_atomic_load(f, __ATOMIC_RELAXED, __HIP_MEMORY_SCOPE_AGENT) >= need) { asm volatile("" ::: "memory"); return true; }
        __builtin_amdgcn_s_sleep(2);
    }
    return false;
}

__device__ __forceinline__ bool acquire_flags(unsigned char* ws, unsigned char* lds, int f0, unsigned n0, int f1, unsigned n1, int f2, unsigned n2) {
    volatile int& okw = *reinterpret_cast<volatile int*>(lds + 152 * 1024);
    if (threadIdx.x < 64) {
        bool ok = wait_flag(ws, f0, n0);
        if (ok && f1 >= 0) ok = wait_flag(ws, f1, n1);
        if (ok && f2 >= 0) ok = wait_flag(ws, f2, n2);
        __builtin_amdgcn_fence(__ATOMIC_ACQUIRE, "agent");
        asm volatile("s_waitcnt vmcnt(0)" ::: "memory");
        if (threadIdx.x == 0) okw = ok ? 1 : 0;
    }
    __syncthreads();
    const bool r = okw != 0;
    __syncthreads();
    return r;
}

template <int HPW>
__device__ __forceinline__ void item_px(unsigned char* ws, int s, int hq) {
    int tid = threadIdx.x;
    asm volatile("" : "+v"(tid));
    const bf16_t* QK = (const bf16_t*)(ws + OFF_QK);
    const bf16_t* Km = (const bf16_t*)(ws + OFF_KM);
    auto rpx = __builtin_amdgcn_make_buffer_rsrc(ws + OFF_PX, 0, (unsigned)((size_t)S_ * KZ * 2), 0x00020000);
    const int lane = tid & 63, wv = tid >> 6, fr = lane & 15, g = lane >> 4;
    bf16x8 fa[HPW][6][3], fb[HPW][6][2];
#pragma unroll
    for (int hi = 0; hi < HPW; ++hi)
#pragma unroll
        for (int ks = 0; ks < 6; ++ks) {
            const int e = ((hq * 4 + wv) * HPW + hi) * DH + ks * 32 + g * 8;
            fa[hi][ks][0] = *reinterpret_cast<const bf16x8*>(Km + (size_t)(s * MM + (fr & 7)) * HD + e);
            fa[hi][ks][1] = *reinterpret_cast<const bf16x8*>(QK + (size_t)(s * SEG + fr) * (2 * HD) + HD + e);
            fa[hi][ks][2] = *reinterpret_cast<const bf16x8*>(QK + (size_t)(s * SEG + 16 + fr) * (2 * HD) + HD + e);
            fb[hi][ks][0] = *reinterpret_cast<const bf16x8*>(QK + (size_t)(s * SEG + fr) * (2 * HD) + e);
            fb[hi][ks][1] = *reinterpret_cast<const bf16x8*>(QK + (size_t)(s * SEG + 16 + fr) * (2 * HD) + e);
        }
    __builtin_amdgcn_sched_barrier(0);
#pragma unroll
    for (int hi = 0; hi < HPW; ++hi) {
        const int h = (hq * 4 + wv) * HPW + hi;
        f32x4 sc[3][2] = {};
#pragma unroll
        for (int ks = 0; ks < 6; ++ks)
#pragma unroll
            for (int jt = 0; jt < 3; ++jt)
#pragma unroll
                for (int it = 0; it < 2; ++it) sc[jt][it] = __builtin_amdgcn_mfma_f32_16x16x32_bf16(fa[hi][ks][jt], fb[hi][ks][it], sc[jt][it], 0, 0, 0);
        const float scale = 0.07216878364870323f;
        const float maskv = -998244352.0f;
#pragma unroll
        for (int it = 0; it < 2; ++it) {
            const int i = it * 16 + fr;
            float v[3][4];
#pragma unroll
            for (int jt = 0; jt < 3; ++jt)
#pragma unroll
                for (int r = 0; r < 4; ++r) {
                    float x = rbf(rbf(sc[jt][it][r]) * scale);
                    if (jt > 0) { const int jx = (jt - 1) * 16 + 4 * g + r; if (jx > i) x = maskv; }
                    v[jt][r] = x;
                }
            if (g >= 2) { v[0][0] = v[0][1] = v[0][2] = v[0][3] = -__builtin_inff(); }
            float mx = v[0][0];
#pragma unroll
            for (int jt = 0; jt < 3; ++jt)
#pragma unroll
                for (int r = 0; r < 4; ++r) mx = fmaxf(mx, v[jt][r]);
            mx = fmaxf(mx, __shfl_xor(mx, 16));
            mx = fmaxf(mx, __shfl_xor(mx, 32));
            float sum = 0.f;
#pragma unroll
            for (int jt = 0; jt < 3; ++jt)
#pragma unroll
                for (int r = 0; r < 4; ++r) { v[jt][r] = __expf(v[jt][r] - mx); sum += v[jt][r]; }
            sum += __shfl_xor(sum, 16);
            sum += __shfl_xor(sum, 32);
            const float inv = 1.0f / sum;
#pragma unroll
            for (int jt = 0; jt < 3; ++jt) {
                v2u o; o.x = pack2(v[jt][0] * inv, v[jt][1] * inv); o.y = pack2(v[jt][2] * inv, v[jt][3] * inv);
                const int k = jt == 0 ? h * 8 + 4 * g : 256 + h * 32 + (jt - 1) * 16 + 4 * g;
                if (jt > 0 || g < 2) __builtin_amdgcn_raw_buffer_store_b64(o, rpx, ((s * SEG + i) * KZ + k) * 2, 0, 16);
            }
        }
    }
}

__device__ __forceinline__ void item_g4(unsigned char* ws, const bf16_t* __restrict__ x, int s, int cb) {
    int tid = threadIdx.x;
    asm volatile("" : "+v"(tid));
    const bf16_t* Px = (const bf16_t*)(ws + OFF_PX);
    const bf16_t* Ut = (const bf16_t*)(ws + OFF_UT);
    const bf16_t* Dt = (const bf16_t*)(ws + OFF_DT);
    auto rh = __builtin_amdgcn_make_buffer_rsrc(ws + OFF_H, 0, (unsigned)((size_t)S_ * DM * 2), 0x00020000);
    const int lane = tid & 63, wv = tid >> 6, fr = lane & 15, g = lane >> 4;
    const int col0 = cb * 128 + wv * 32;
    f32x4 acc[2][2] = {};
    const bf16_t* pa0 = Px + (size_t)(s * SEG + fr) * KZ + g * 8;
    const bf16_t* pa1 = pa0 + 16 * KZ;
    const int c0 = (col0 + fr) >> 3, cc0 = (col0 + fr) & 7;
    const bf16_t* ub = Ut + (((size_t)s * NCONS + c0) * NH + g) * 64 + cc0 * 8;
    const bf16_t* db = Dt + (((size_t)s * NCONS + c0) * NH) * 256 + cc0 * 32 + g * 8;
#pragma unroll
    for (int bt = 0; bt < 3; ++bt) {
        bf16x8 a0[14], a1[14], b0[14], b1[14];
#pragma unroll
        for (int i = 0; i < 14; ++i) {
            const int ks = bt * 14 + i;
            if (ks >= 40) continue;
            a0[i] = *reinterpret_cast<const bf16x8*>(pa0 + ks * 32);
            a1[i] = *reinterpret_cast<const bf16x8*>(pa1 + ks * 32);
            const bf16_t* bp = ks < 8 ? ub + (size_t)ks * 4 * 64 : db + (size_t)(ks - 8) * 256;
            const size_t n1 = ks < 8 ? (size_t)2 * NH * 64 : (size_t)2 * NH * 256;
            b0[i] = *reinterpret_cast<const bf16x8*>(bp);
            b1[i] = *reinterpret_cast<const bf16x8*>(bp + n1);
        }
        __builtin_amdgcn_sched_barrier(0);
#pragma unroll
        for (int i = 0; i < 14; ++i) {
            if (bt * 14 + i >= 40) continue;
            acc[0][0] = __builtin_amdgcn_mfma_f32_16x16x32_bf16(b0[i], a0[i], acc[0][0], 0, 0, 0);
            acc[1][0] = __builtin_amdgcn_mfma_f32_16x16x32_bf16(b0[i], a1[i], acc[1][0], 0, 0, 0);
            acc[0][1] = __builtin_amdgcn_mfma_f32_16x16x32_bf16(b1[i], a0[i], acc[0][1], 0, 0, 0);
            acc[1][1] = __builtin_amdgcn_mfma_f32_16x16x32_bf16(b1[i], a1[i], acc[1][1], 0, 0, 0);
        }
        __builtin_amdgcn_sched_barrier(0);
    }
#pragma unroll
    for (int m = 0; m < 2; ++m)
#pragma unroll
        for (int n = 0; n < 2; ++n) {
            const int idx = (s * SEG + m * 16 + fr) * DM + col0 + n * 16 + g * 4;
            const v2u xv = *reinterpret_cast<const v2u*>(x + idx);
            v2u o;
            o.x = pack2(bf2f(xv.x & 0xffff) + rbf(acc[m][n][0]), bf2f(xv.x >> 16) + rbf(acc[m][n][1]));
            o.y = pack2(bf2f(xv.y & 0xffff) + rbf(acc[m][n][2]), bf2f(xv.y >> 16) + rbf(acc[m][n][3]));
            __builtin_amdgcn_raw_buffer_store_b64(o, rh, idx * 2, 0, 16);
        }
}

__device__ __forceinline__ void item_ln(unsigned char* ws, int s) {
    int tid = threadIdx.x;
    asm volatile("" : "+v"(tid));
    auto rhn = __builtin_amdgcn_make_buffer_rsrc(ws + OFF_HN, 0, (unsigned)((size_t)S_ * DM * 2), 0x00020000);
    const int lane = tid & 63, wv = tid >> 6;
#pragma unroll 2
    for (int r = 0; r < 8; ++r) {
        const int row = s * SEG + wv * 8 + r;
        unsigned o[5];
        ln_row_regs((const bf16_t*)(ws + OFF_H) + (size_t)row * DM, lane, o);
#pragma unroll
        for (int i = 0; i < 5; ++i) __builtin_amdgcn_raw_buffer_store_b32(o[i], rhn, (row * DM) * 2 + (lane + 64 * i) * 4, 0, 16);
    }
}

__device__ __forceinline__ void item_g5(unsigned char* ws, const bf16_t* Wg, const bf16_t* Wu, int fb, int bn,
                                        unsigned char* lds) {
    constexpr int BM = 128, BN = 192;
    f32x4 acc[BM / 32][BN / 32] = {};
    gemm_mainloop_ms<BM, BN, true, 3, true>((const bf16_t*)(ws + OFF_HN) + (size_t)fb * BM * DM, DM, Wg + bn * 96, FF, DM, lds, acc,
                                            (long)(Wu - Wg));
    int tid = threadIdx.x;
    asm volatile("" : "+v"(tid));
    auto rgu = __builtin_amdgcn_make_buffer_rsrc(ws + OFF_GU, 0, (unsigned)((size_t)S_ * FF * 2), 0x00020000);
    const int lane = tid & 63, wid = tid >> 6, wr = wid >> 1, wc = wid & 1, fr = lane & 15, fq = lane >> 4;
#pragma unroll
    for (int m = 0; m < 4; ++m)
#pragma unroll
        for (int n = 0; n < 3; ++n) {
            float r[4];
#pragma unroll
            for (int j = 0; j < 4; ++j) r[j] = rbf(acc[m][2 * n][j]) * rbf(acc[m][2 * n + 1][j]);
            v2u o; o.x = pack2(r[0], r[1]); o.y = pack2(r[2], r[3]);
            const int tok = fb * BM + wr * 64 + m * 16 + fr, f = bn * 96 + wc * 48 + n * 16 + fq * 4;
            __builtin_amdgcn_raw_buffer_store_b64(o, rgu, (tok * FF + f) * 2, 0, 16);
        }
}

__device__ __forceinline__ void item_g6(unsigned char* ws, const bf16_t* __restrict__ Wd, bf16_t* __restrict__ out, int fb, int bn,
                                        unsigned char* lds) {
    constexpr int BM = 128, BN = 128;
    f32x4 acc[4][4] = {};
    gemm_mainloop_ms<BM, BN, true, 4>((const bf16_t*)(ws + OFF_GU) + (size_t)fb * BM * FF, FF, Wd + bn * BN, DM, FF, lds, acc);
    int tid = threadIdx.x;
    asm volatile("" : "+v"(tid));
    const bf16_t* H = (const bf16_t*)(ws + OFF_H);
    const int lane = tid & 63, wid = tid >> 6, wr = wid >> 1, wc = wid & 1, fr = lane & 15, fq = lane >> 4;
#pragma unroll
    for (int m = 0; m < 4; ++m)
#pragma unroll
        for (int n = 0; n < 4; ++n) {
            const size_t idx = (size_t)(fb * BM + wr * 64 + m * 16 + fr) * DM + bn * BN + wc * 64 + n * 16 + fq * 4;
            const v2u hv = *reinterpret_cast<const v2u*>(H + idx);
            v2u o;
            o.x = pack2(bf2f(hv.x & 0xffff) + rbf(acc[m][n][0]), bf2f(hv.x >> 16) + rbf(acc[m][n][1]));
            o.y = pack2(bf2f(hv.y & 0xffff) + rbf(acc[m][n][2]), bf2f(hv.y >> 16) + rbf(acc[m][n][3]));
            *reinterpret_cast<v2u*>(out + idx) = o;
        }
}

constexpr int QK_NA = 64, QK_NV = 36, QK_NLDS = 20;
constexpr int MROW = 1344;
constexpr int L_W = 0;
constexpr int L_MEM = 4 * QK_NLDS * 1024;
constexpr int L_SC = L_MEM + 8 * MROW;
constexpr int L_U = 0;
constexpr int REC_LDS = L_SC + 4 * 3 * 1024;
static_assert(REC_LDS <= 160 * 1024, "lds");
constexpr int SPIN_CAP = 1 << 20;
#define DLY_P 130
#define DLY_M 240
#define DLY_C 210
__device__ __forceinline__ void quiet_until(unsigned long long t0, int d) {
    if (d > 0) while ((long long)(__builtin_amdgcn_s_memrealtime() - t0) < d) __builtin_amdgcn_s_sleep(1);
}

__device__ __forceinline__ void mfma_a0(f32x4& acc, const bf16x8& a, const bf16x8& b) {
    asm volatile("v_mfma_f32_16x16x32_bf16 %0, %1, %2, 0" : "=&v"(acc) : "a"(a), "v"(b));
}
__device__ __forceinline__ void mfma_a(f32x4& acc, const bf16x8& a, const bf16x8& b) {
    asm volatile("v_mfma_f32_16x16x32_bf16 %0, %1, %2, %0" : "+v"(acc) : "a"(a), "v"(b));
}
__device__ __forceinline__ void mfma_v0(f32x4& acc, const bf16x8& a, const bf16x8& b) {
    asm volatile("v_mfma_f32_16x16x32_bf16 %0, %1, %2, 0" : "=&v"(acc) : "v"(a), "v"(b));
}
__device__ __forceinline__ void mfma_v(f32x4& acc, const bf16x8& a, const bf16x8& b) {
    asm volatile("v_mfma_f32_16x16x32_bf16 %0, %1, %2, %0" : "+v"(acc) : "v"(a), "v"(b));
}

__device__ __forceinline__ void bar_lds() { asm volatile("s_waitcnt lgkmcnt(0)\n\ts_barrier" ::: "memory"); }

__device__ __forceinline__ bool valid4(v4u v) {
    return (v.x != 0xFFFFFFFFu) & (v.y != 0xFFFFFFFFu) & (v.z != 0xFFFFFFFFu) & (v.w != 0xFFFFFFFFu);
}

template <int N>
__device__ __forceinline__ bool poll2(__amdgpu_buffer_rsrc_t r, const int (&off)[N], v4u (&out)[N]) {
    for (int spins = 0; spins < SPIN_CAP; ++spins) {
        int z = 0;
        asm volatile("" : "+v"(z) :: "memory");
#pragma unroll
        for (int i = 0; i < N; ++i) out[i] = __builtin_amdgcn_raw_buffer_load_b128(r, off[i] + z, 0, 16);
        unsigned mx = 0;
#pragma unroll
        for (int i = 0; i < N; ++i) mx = max(max(max(mx, out[i].x), max(out[i].y, out[i].z)), out[i].w);
        if (__all(mx != 0xFFFFFFFFu)) return true;
    }
    return false;
}

__device__ __forceinline__ bool sweep_mem(__amdgpu_buffer_rsrc_t rm, int s, unsigned char* lds, int wave, int lane) {
    v4u v[3];
    int off[3];
#pragma unroll
    for (int i = 0; i < 3; ++i) {
        const int ch = wave * 160 + i * 64 + (i == 2 ? lane & 31 : lane);
        off[i] = s * (int)MS_BYTES + ch * 16;
    }
    if (!poll2<3>(rm, off, v)) return false;
#pragma unroll
    for (int i = 0; i < 3; ++i) {
        const int ch = wave * 160 + i * 64 + lane;
        if (i < 2 || lane < 32) *reinterpret_cast<v4u*>(lds + L_MEM + (ch & 7) * MROW + (ch >> 3) * 16) = v[i];
    }
    return true;
}

__global__ __launch_bounds__(256, 1) void k_rec(const bf16_t* __restrict__ Wq, const bf16_t* __restrict__ Wk,
                                                const bf16_t* __restrict__ Wv, const bf16_t* __restrict__ Wo,
                                                const bf16_t* Wg, const bf16_t* Wu,
                                                const bf16_t* __restrict__ Wd, const bf16_t* __restrict__ x,
                                                bf16_t* __restrict__ out, unsigned char* ws) {
    extern __shared__ __attribute__((aligned(16))) unsigned char lds[];
    const int tid = threadIdx.x, lane = tid & 63, w = __builtin_amdgcn_readfirstlane(tid >> 6);
    const int fr = lane & 15, g = lane >> 4;
    auto rm = __builtin_amdgcn_make_buffer_rsrc(ws + OFF_MS, 0, (unsigned)((NSEG + 1) * MS_BYTES), 0x00020000);
    auto rp = __builtin_amdgcn_make_buffer_rsrc(ws + OFF_PS, 0, (unsigned)(NSEG * PS_BYTES), 0x00020000);
    auto rqk = __builtin_amdgcn_make_buffer_rsrc(ws + OFF_QK, 0, (unsigned)((size_t)S_ * 2 * HD * 2), 0x00020000);
    auto rdt = __builtin_amdgcn_make_buffer_rsrc(ws + OFF_DT, 0, (unsigned)((size_t)NSEG * DM * NH * SEG * 2), 0x00020000);
    unsigned* err = reinterpret_cast<unsigned*>(ws + OFF_ERR);
    const bf16_t* XN = (const bf16_t*)(ws + OFF_XN);
    const bf16_t* Cf = (const bf16_t*)(ws + OFF_CF);
    const int bid = blockIdx.x;
    if (bid >= NH + NCONS) {
        constexpr int NW = 256 - NH - NCONS;
        constexpr int N1 = 0, PER_FB = 61;
        {
            const int wk = bid - (NH + NCONS);
            for (int e = wk >> 3; e < 208; e += NW / 8) {
                const int xg = wk & 7;
                if (e < 144) {
                    const bool small = e < 32;
                    const int blk = small ? e >> 3 : 2 + ((e - 32) >> 3), j = e & 7, h = xg + 8 * (j & 3);
                    if (j < 4) {
                        if (small) tile_vd<64>(XN, Wv, Wo, rdt, blk, h, lds); else tile_vd<128>(XN, Wv, Wo, rdt, blk, h, lds);
                        publish_add(ws, small ? FL_D0 + blk : FL_D + (blk >> 1));
                    } else {
                        if (small) tile_k<64>(XN, Wk + h * DH, rqk, blk, HD + h * DH, lds); else tile_k<128>(XN, Wk + h * DH, rqk, blk, HD + h * DH, lds);
                        publish_add(ws, small ? FL_K0 + blk * NH + h : FL_K + (blk >> 1) * NH + h);
                    }
                } else {
                    const int hb = (e - 144) >> 2, h = xg + 8 * (e & 3);
                    tile_k<128>(XN, Wq + h * DH, rqk, hb, h * DH, lds);
                    publish_add(ws, FL_Q + (hb >> 1));
                }
            }
        }
        for (int it = bid - (NH + NCONS); it < N1 + FIN * PER_FB; it += NW) {
            {
                int r = it - N1, f0 = 0, nf = FIN < 8 ? FIN : 8;
                if (r >= 8 * PER_FB) { r -= 8 * PER_FB; f0 = 8; nf = FIN - 8; }
                bool ok = true;
                if (r < nf * 16) {
                    const int s = f0 * 4 + (r >> 2);
                    ok = acquire_flags(ws, lds, FL_KM + s, NH, FL_Q + (s >> 3), 2 * NH, -1, 0);
                    if (ok) { item_px<2>(ws, s, r & 3); publish_add(ws, FL_PX + s); }
                } else if (r < nf * 36) {
                    r -= nf * 16;
                    const int s = f0 * 4 + r / 5, cb = r % 5;
                    ok = acquire_flags(ws, lds, FL_PX + s, 4, FL_U + s, NCONS, -1, 0);
                    if (ok) { item_g4(ws, x, s, cb); publish_add(ws, FL_H + s); }
                } else if (r < nf * 40) {
                    const int s = f0 * 4 + (r - nf * 36);
                    ok = acquire_flags(ws, lds, FL_H + s, 5, -1, 0, -1, 0);
                    if (ok) { item_ln(ws, s); publish_add(ws, FL_LN + (s >> 2)); }
                } else if (r < nf * 56) {
                    r -= nf * 40;
                    const int fb = f0 + (r >> 4);
                    ok = acquire_flags(ws, lds, FL_LN + fb, 4, -1, 0, -1, 0);
                    if (ok) { item_g5(ws, Wg, Wu, fb, r & 15, lds); publish_add(ws, FL_G5 + fb); }
                } else {
                    r -= nf * 56;
                    const int fb = f0 + r / 5;
                    ok = acquire_flags(ws, lds, FL_G5 + fb, 16, -1, 0, -1, 0);
                    if (ok) { item_g6(ws, Wd, out, fb, r % 5, lds); __syncthreads(); }
                }
                if (!ok) { if (threadIdx.x == 0) atomicAdd(err, 1u); return; }
            }
        }
        return;
    }
    const unsigned char* memrd = lds + L_MEM + (fr & 7) * MROW + g * 16;

    if (bid < NH) {
        const int h = bid;
        auto rkm = __builtin_amdgcn_make_buffer_rsrc(ws + OFF_KM, 0, (unsigned)((size_t)NSEG * MM * HD * 2), 0x00020000);
        bf16x8 wa[QK_NA], wv[QK_NV];
        {
            unsigned char* stg = lds + 112 * 1024;
#pragma unroll
            for (int blk = 0; blk < 10; ++blk) {
                stage_kn<192>(Wq + h * DH, HD, blk, stg, tid);
                stage_kn<192>(Wk + h * DH, HD, blk, stg + 24 * 1024, tid);
                asm volatile("s_waitcnt vmcnt(0)" ::: "memory");
                __syncthreads();
#pragma unroll
                for (int t = 0; t < 6; ++t)
#pragma unroll
                    for (int kk = 0; kk < 2; ++kk) {
                        const int f = t * 20 + blk * 2 + kk;
                        const bf16x8 v = frag_kn<192>(stg + (t >= 3 ? 24 * 1024 : 0), w * 3 + t % 3, kk, lane);
                        if (f < QK_NA) wa[f] = v;
                        else if (f < QK_NA + QK_NV) wv[f - QK_NA] = v;
                        else *reinterpret_cast<bf16x8*>(lds + L_W + (w * QK_NLDS + f - QK_NA - QK_NV) * 1024 + lane * 16) = v;
                    }
                __syncthreads();
            }
        }
        const unsigned char* wlrd = lds + L_W + w * QK_NLDS * 1024 + lane * 16;
        const float scale = 0.07216878364870323f;
        bf16x4 kx[2][3];
        auto load_kx = [&](int s) {
#pragma unroll
            for (int jt = 0; jt < 2; ++jt)
#pragma unroll
                for (int n = 0; n < 3; ++n)
                    kx[jt][n] = __builtin_bit_cast(bf16x4, __builtin_amdgcn_raw_buffer_load_b64(rqk, ((s * SEG + jt * 16 + fr) * (2 * HD) + HD + h * DH + (w * 3 + n) * 16 + g * 4) * 2, 0, 16));
        };
        if (!wait_flag(ws, FL_K0 + h, 1u)) { if (lane == 0) atomicAdd(err, 1u); return; }
        load_kx(0);
        unsigned long long tq = 0;
        for (int s = 0; s < NSEG; ++s) {
            if (s > 0) quiet_until(tq, DLY_M);
            if (!sweep_mem(rm, s, lds, w, lane)) { if (lane == 0) atomicAdd(err, 1u); return; }
            asm volatile("s_waitcnt vmcnt(0)" ::: "memory");
            bar_lds();
            if (s > 0 && tid == 64)
                __hip_atomic_fetch_add((gu32*)(ws + OFF_FL + (size_t)(FL_KM + s - 1) * 128), 1u, __ATOMIC_RELAXED, __HIP_MEMORY_SCOPE_AGENT);
            f32x4 acc[6];
            {
                bf16x8 bq[3], wl[3];
                bq[0] = *reinterpret_cast<const bf16x8*>(memrd); wl[0] = *reinterpret_cast<const bf16x8*>(wlrd);
                bq[1] = *reinterpret_cast<const bf16x8*>(memrd + 64); wl[1] = *reinterpret_cast<const bf16x8*>(wlrd + 1024);
#pragma unroll
                for (int m = 0; m < 20; ++m) {
                    if (m + 2 < 20) {
                        bq[(m + 2) % 3] = *reinterpret_cast<const bf16x8*>(memrd + (m + 2) * 64);
                        wl[(m + 2) % 3] = *reinterpret_cast<const bf16x8*>(wlrd + (m + 2) * 1024);
                    }
                    __builtin_amdgcn_sched_barrier(0);
#pragma unroll
                    for (int t = 0; t < 5; ++t) {
                        const int f = t * 20 + m;
                        if (f < QK_NA) { if (m == 0) mfma_a0(acc[t], wa[f], bq[m % 3]); else mfma_a(acc[t], wa[f], bq[m % 3]); }
                        else { if (m == 0) mfma_v0(acc[t], wv[f - QK_NA], bq[m % 3]); else mfma_v(acc[t], wv[f - QK_NA], bq[m % 3]); }
                    }
                    if (m == 0) mfma_v0(acc[5], wl[0], bq[0]); else mfma_v(acc[5], wl[m % 3], bq[m % 3]);
                    __builtin_amdgcn_sched_barrier(0);
                }
                asm volatile("s_nop 7\n\ts_nop 3" : "+v"(acc[0]), "+v"(acc[1]), "+v"(acc[2]), "+v"(acc[3]), "+v"(acc[4]), "+v"(acc[5]));
            }
            bf16x4 q4[3], k4[3];
#pragma unroll
            for (int n = 0; n < 3; ++n) {
                v2u a; a.x = pack2(acc[n][0], acc[n][1]); a.y = pack2(acc[n][2], acc[n][3]);
                q4[n] = __builtin_bit_cast(bf16x4, a);
                v2u b; b.x = pack2(acc[3 + n][0], acc[3 + n][1]); b.y = pack2(acc[3 + n][2], acc[3 + n][3]);
                k4[n] = __builtin_bit_cast(bf16x4, b);
            }
            if (fr < 8) {
#pragma unroll
                for (int n = 0; n < 3; ++n)
                    __builtin_amdgcn_raw_buffer_store_b64(__builtin_bit_cast(v2u, k4[n]), rkm, ((s * MM + fr) * HD + h * DH + (w * 3 + n) * 16 + g * 4) * 2, 0, 16);
            }
            if (s == NSEG - 1) { publish_add(ws, FL_KM + s); break; }
            f32x4 sc[3] = {};
#pragma unroll
            for (int n = 0; n < 3; ++n) {
                sc[0] = __builtin_amdgcn_mfma_f32_16x16x16bf16_1k(k4[n], q4[n], sc[0], 0, 0, 0);
                sc[1] = __builtin_amdgcn_mfma_f32_16x16x16bf16_1k(kx[0][n], q4[n], sc[1], 0, 0, 0);
                sc[2] = __builtin_amdgcn_mfma_f32_16x16x16bf16_1k(kx[1][n], q4[n], sc[2], 0, 0, 0);
            }
            if (fr < 8) {
                unsigned char* prow = lds + L_SC + (w * 8 + fr) * 176;
                if (g < 2) *reinterpret_cast<f32x4*>(prow + g * 16) = sc[0];
                *reinterpret_cast<f32x4*>(prow + 32 + g * 16) = sc[1];
                *reinterpret_cast<f32x4*>(prow + 96 + g * 16) = sc[2];
            }
            bar_lds();
            if (w == 0) {
                const int t = lane >> 3, q = lane & 7;
                const unsigned char* prow = lds + L_SC + t * 176;
                f32x4 xs = *reinterpret_cast<const f32x4*>(prow + 32 + q * 16);
                float ms = *reinterpret_cast<const float*>(prow + q * 4);
                f32x4 x1 = *reinterpret_cast<const f32x4*>(prow + 8 * 176 + 32 + q * 16);
                float m1 = *reinterpret_cast<const float*>(prow + 8 * 176 + q * 4);
                f32x4 x2 = *reinterpret_cast<const f32x4*>(prow + 16 * 176 + 32 + q * 16);
                float m2 = *reinterpret_cast<const float*>(prow + 16 * 176 + q * 4);
                f32x4 x3 = *reinterpret_cast<const f32x4*>(prow + 24 * 176 + 32 + q * 16);
                float m3 = *reinterpret_cast<const float*>(prow + 24 * 176 + q * 4);
                float v[5];
#pragma unroll
                for (int r = 0; r < 4; ++r) v[r] = rbf(rbf((xs[r] + x1[r]) + (x2[r] + x3[r])) * scale);
                v[4] = rbf(rbf((ms + m1) + (m2 + m3)) * scale);
                float mx = fmaxf(fmaxf(fmaxf(v[0], v[1]), fmaxf(v[2], v[3])), v[4]);
                mx = fmaxf(mx, dppf<0xB1>(mx));
                mx = fmaxf(mx, dppf<0x4E>(mx));
                mx = fmaxf(mx, dppf<0x141>(mx));
                float sum = 0.f;
#pragma unroll
                for (int r = 0; r < 5; ++r) { v[r] = __expf(v[r] - mx); sum += v[r]; }
                sum += dppf<0xB1>(sum);
                sum += dppf<0x4E>(sum);
                sum += dppf<0x141>(sum);
                const float inv = __builtin_amdgcn_rcpf(sum);
                v2u o; o.x = sanit(pack2(v[0] * inv, v[1] * inv)); o.y = sanit(pack2(v[2] * inv, v[3] * inv));
                __builtin_amdgcn_raw_buffer_store_b64(o, rp, s * (int)PS_BYTES + h * 640 + 128 + t * 64 + q * 8, 0, 16);
                const unsigned pm = f2bf(v[4] * inv);
                const unsigned p1 = dppu<0x55>(pm), p2 = dppu<0xAA>(pm), p3 = dppu<0xFF>(pm);
                v2u om; om.x = sanit(pm | (p1 << 16)); om.y = sanit(p2 | (p3 << 16));
                if ((q & 3) == 0) __builtin_amdgcn_raw_buffer_store_b64(om, rp, s * (int)PS_BYTES + h * 640 + t * 16 + q * 2, 0, 16);
            }
            tq = __builtin_amdgcn_s_memrealtime() - (w == 0 ? 35 : 0);
            if (((s + 1) & 7) == 0 && !wait_flag(ws, FL_K + ((s + 1) >> 3) * NH + h, 2u)) { if (lane == 0) atomicAdd(err, 1u); return; }
            if (s + 1 < 8 && ((s + 1) & 1) == 0 && !wait_flag(ws, FL_K0 + ((s + 1) >> 1) * NH + h, 1u)) { if (lane == 0) atomicAdd(err, 1u); return; }
            load_kx(s + 1);
        }
    } else {
        const int c = bid - NH;
        auto rut = __builtin_amdgcn_make_buffer_rsrc(ws + OFF_UT, 0, (unsigned)((size_t)NSEG * DM * NH * MM * 2), 0x00020000);
        bf16x8 ca[64], cv[16];
        {
            const bf16_t* cbase = Cf + ((size_t)(c * 4 + w) * 80 * 64 + lane) * 8;
#pragma unroll
            for (int f = 0; f < 80; ++f) {
                const bf16x8 v = *reinterpret_cast<const bf16x8*>(cbase + (size_t)f * 512);
                if (f < 64) ca[f] = v; else cv[f - 64] = v;
            }
        }
        bf16x8 df[5];
        auto load_d = [&](int s) {
#pragma unroll
            for (int i = 0; i < 5; ++i) {
                const int ks = w * 10 + 2 * i + (fr >> 3);
                const int hh = ks >= 8 ? ks - 8 : 0;
                df[i] = __builtin_bit_cast(bf16x8, __builtin_amdgcn_raw_buffer_load_b128(rdt, ((((s * NCONS + c) * NH + hh) * 8 + (fr & 7)) * 32 + g * 8) * 2, 0, 16));
            }
        };
        if (!wait_flag(ws, FL_D0, (unsigned)NH)) { if (lane == 0) atomicAdd(err, 1u); return; }
        load_d(0);
        unsigned long long tcm = 0;
        int poff[5];
#pragma unroll
        for (int i = 0; i < 5; ++i) {
            const int ks = w * 10 + 2 * i + (fr >> 3);
            poff[i] = ks < 8 ? (4 * ks + g) * 640 + (fr & 7) * 16 : (ks - 8) * 640 + 128 + (fr & 7) * 64 + g * 16;
        }
        for (int s = 0; s < NSEG; ++s) {
            if (s > 0) quiet_until(tcm, DLY_C);
            if (!sweep_mem(rm, s, lds, w, lane)) { if (lane == 0) atomicAdd(err, 1u); return; }
            const unsigned long long tc = DLY_P > 0 ? __builtin_amdgcn_s_memrealtime() : 0ull;
            bar_lds();
            f32x4 ua[4];
            {
                bf16x8 bq[3];
                bq[0] = *reinterpret_cast<const bf16x8*>(memrd);
                bq[1] = *reinterpret_cast<const bf16x8*>(memrd + 64);
#pragma unroll
                for (int m = 0; m < 20; ++m) {
                    if (m + 2 < 20) bq[(m + 2) % 3] = *reinterpret_cast<const bf16x8*>(memrd + (m + 2) * 64);
                    __builtin_amdgcn_sched_barrier(0);
#pragma unroll
                    for (int t = 0; t < 4; ++t) {
                        const int f = t * 20 + m;
                        if (f < 64) { if (m == 0) mfma_a0(ua[t], ca[f], bq[m % 3]); else mfma_a(ua[t], ca[f], bq[m % 3]); }
                        else { if (m == 0) mfma_v0(ua[t], cv[f - 64], bq[m % 3]); else mfma_v(ua[t], cv[f - 64], bq[m % 3]); }
                    }
                    __builtin_amdgcn_sched_barrier(0);
                }
                asm volatile("s_nop 7\n\ts_nop 3" : "+v"(ua[0]), "+v"(ua[1]), "+v"(ua[2]), "+v"(ua[3]));
            }
            if (fr < 8) {
#pragma unroll
                for (int t = 0; t < 4; ++t) {
                    const int hh = 2 * (4 * w + t) + (g >> 1);
#pragma unroll
                    for (int r = 0; r < 4; ++r)
                        *reinterpret_cast<bf16_t*>(lds + L_U + ((hh * 8 + 4 * (g & 1) + r) * 8 + fr) * 2) = f2bf(ua[t][r]);
                }
            }
            bar_lds();
            __builtin_amdgcn_raw_buffer_store_b128(*reinterpret_cast<const v4u*>(lds + L_U + tid * 16), rut, ((s * NCONS + c) * 2048 + tid * 8) * 2, 0, 16);
            if (s == NSEG - 1) { publish_add(ws, FL_U + s); break; }
            if (w == 0) {
#pragma unroll
                for (int i = 0; i < 4; ++i) df[i] = *reinterpret_cast<const bf16x8*>(lds + L_U + (((4 * (2 * i + (fr >> 3)) + g) * 8 + (fr & 7)) * 8) * 2);
            }
            quiet_until(tc, DLY_P);
            v4u pf[5];
            {
                int po[5];
#pragma unroll
                for (int i = 0; i < 5; ++i) po[i] = s * (int)PS_BYTES + poff[i];
                if (!poll2<5>(rp, po, pf)) { if (lane == 0) atomicAdd(err, 1u); return; }
            }
            f32x4 fa = {};
#pragma unroll
            for (int i = 0; i < 5; ++i) fa = __builtin_amdgcn_mfma_f32_16x16x32_bf16(df[i], __builtin_bit_cast(bf16x8, pf[i]), fa, 0, 0, 0);
            *reinterpret_cast<f32x4*>(lds + L_SC + (w * 64 + lane) * 16) = fa;
            asm volatile("s_waitcnt vmcnt(0)" ::: "memory");
            bar_lds();
            if (w == 0) {
                const int l0 = (lane & 31), l1 = ((lane & 31) | 40) ;
                f32x4 pa[4], pb[4];
#pragma unroll
                for (int i = 0; i < 4; ++i) {
                    pa[i] = *reinterpret_cast<const f32x4*>(lds + L_SC + (i * 64 + l0) * 16);
                    pb[i] = *reinterpret_cast<const f32x4*>(lds + L_SC + (i * 64 + l1) * 16);
                }
                __builtin_amdgcn_sched_barrier(0);
                const f32x4 t0 = pa[0] + pb[0], t1 = pa[1] + pb[1], t2 = pa[2] + pb[2], t3 = pa[3] + pb[3];
                const f32x4 tt = (t0 + t1) + (t2 + t3);
                v2u o;
                o.x = sanit(pack2(tt[0], tt[1]));
                o.y = sanit(pack2(tt[2], tt[3]));
                if (fr < 8 && g < 2)
                    __builtin_amdgcn_raw_buffer_store_b64(o, rm, (s + 1) * (int)MS_BYTES + ((c * 8 + fr) * 8 + 4 * g) * 2, 0, 16);
                if (lane == 0)
                    __hip_atomic_fetch_add((gu32*)(ws + OFF_FL + (size_t)(FL_U + s) * 128), 1u, __ATOMIC_RELAXED, __HIP_MEMORY_SCOPE_AGENT);
            }
            tcm = __builtin_amdgcn_s_memrealtime() - (w == 0 ? 15 : 0);
            if (((s + 1) & 7) == 0 && !wait_flag(ws, FL_D + ((s + 1) >> 3), 2u * NH)) { if (lane == 0) atomicAdd(err, 1u); return; }
            if (s + 1 < 8 && ((s + 1) & 1) == 0 && !wait_flag(ws, FL_D0 + ((s + 1) >> 1), (unsigned)NH)) { if (lane == 0) atomicAdd(err, 1u); return; }
            load_d(s + 1);
        }
    }
}

__global__ __launch_bounds__(256) void k_t_px(unsigned char* ws) { item_px<1>(ws, FIN * 4 + (blockIdx.x >> 3), blockIdx.x & 7); }

__global__ __launch_bounds__(256) void k_t_g4(unsigned char* ws, const bf16_t* __restrict__ x) {
    __shared__ __attribute__((aligned(16))) float red[4][8][64][4];
    const int s = FIN * 4 + blockIdx.x / 10, cb = blockIdx.x % 10;
    const bf16_t* Px = (const bf16_t*)(ws + OFF_PX);
    const bf16_t* Ut = (const bf16_t*)(ws + OFF_UT);
    const bf16_t* Dt = (const bf16_t*)(ws + OFF_DT);
    bf16_t* H = (bf16_t*)(ws + OFF_H);
    const int lane = threadIdx.x & 63, wv = threadIdx.x >> 6, fr = lane & 15, g = lane >> 4;
    const int col0 = cb * 64;
    const bf16_t* pa0 = Px + (size_t)(s * SEG + fr) * KZ + g * 8 + wv * 320;
    const int c0 = (col0 + fr) >> 3, cc0 = (col0 + fr) & 7;
    bf16x8 a[10][2], b[10][4];
#pragma unroll
    for (int i = 0; i < 10; ++i) {
        const int ks = wv * 10 + i;
        a[i][0] = *reinterpret_cast<const bf16x8*>(pa0 + i * 32);
        a[i][1] = *reinterpret_cast<const bf16x8*>(pa0 + 16 * KZ + i * 32);
#pragma unroll
        for (int n = 0; n < 4; ++n) {
            const bf16_t* bp = ks < 8 ? Ut + (((size_t)s * NCONS + c0 + 2 * n) * NH + 4 * ks + g) * 64 + cc0 * 8
                                      : Dt + (((size_t)s * NCONS + c0 + 2 * n) * NH + (ks - 8)) * 256 + cc0 * 32 + g * 8;
            b[i][n] = *reinterpret_cast<const bf16x8*>(bp);
        }
    }
    f32x4 acc[2][4] = {};
#pragma unroll
    for (int i = 0; i < 10; ++i)
#pragma unroll
        for (int m = 0; m < 2; ++m)
#pragma unroll
            for (int n = 0; n < 4; ++n) acc[m][n] = __builtin_amdgcn_mfma_f32_16x16x32_bf16(b[i][n], a[i][m], acc[m][n], 0, 0, 0);
#pragma unroll
    for (int m = 0; m < 2; ++m)
#pragma unroll
        for (int n = 0; n < 4; ++n) *reinterpret_cast<f32x4*>(&red[wv][m * 4 + n][lane][0]) = acc[m][n];
    __syncthreads();
#pragma unroll
    for (int q = 0; q < 2; ++q) {
        const int t = wv * 2 + q, m = t >> 2, n = t & 3;
        const f32x4 r = (*reinterpret_cast<const f32x4*>(&red[0][t][lane][0]) + *reinterpret_cast<const f32x4*>(&red[1][t][lane][0])) +
                        (*reinterpret_cast<const f32x4*>(&red[2][t][lane][0]) + *reinterpret_cast<const f32x4*>(&red[3][t][lane][0]));
        const int idx = (s * SEG + m * 16 + fr) * DM + col0 + n * 16 + g * 4;
        const v2u xv = *reinterpret_cast<const v2u*>(x + idx);
        v2u o;
        o.x = pack2(bf2f(xv.x & 0xffff) + rbf(r[0]), bf2f(xv.x >> 16) + rbf(r[1]));
        o.y = pack2(bf2f(xv.y & 0xffff) + rbf(r[2]), bf2f(xv.y >> 16) + rbf(r[3]));
        *reinterpret_cast<v2u*>(H + idx) = o;
    }
}

__global__ __launch_bounds__(256) void k_t_g5(unsigned char* ws, const bf16_t* Wg, const bf16_t* Wu) {
    extern __shared__ __attribute__((aligned(16))) unsigned char lds[];
    constexpr int BN = 192, NS = 3, BB = BN * 128, AOFF = NS * BB, LPT = BN / 32;
    const int rb = FIN * 2 + (blockIdx.x >> 4), bn = blockIdx.x & 15;
    const int tid = threadIdx.x, lane = tid & 63, wid = tid >> 6, wr = wid >> 1, wc = wid & 1, fr = lane & 15, fq = lane >> 4;
    const bf16_t* B = Wg + bn * 96;
    const long d2 = (long)(Wu - Wg);
    stage_kn<BN, true>(B, FF, 0, lds, tid, d2);
    stage_kn<BN, true>(B, FF, 1, lds + BB, tid, d2);
    {
        const int li = lane & 15;
        v4u hv[4][5];
#pragma unroll
        for (int r4 = 0; r4 < 4; ++r4) {
            const bf16_t* src = (const bf16_t*)(ws + OFF_H) + (size_t)(rb * 64 + wid * 16 + r4 * 4 + fq) * DM;
#pragma unroll
            for (int i = 0; i < 5; ++i) hv[r4][i] = *reinterpret_cast<const v4u*>(src + (li + 16 * i) * 8);
        }
#pragma unroll
        for (int r4 = 0; r4 < 4; ++r4) {
            const int row = wid * 16 + r4 * 4 + fq;
            float v[40];
#pragma unroll
            for (int i = 0; i < 5; ++i)
#pragma unroll
                for (int j = 0; j < 4; ++j) { v[i * 8 + 2 * j] = bf2f(hv[r4][i][j] & 0xffff); v[i * 8 + 2 * j + 1] = bf2f(hv[r4][i][j] >> 16); }
            float sum = 0.f;
#pragma unroll
            for (int i = 0; i < 40; ++i) sum += v[i];
            sum += dppf<0xB1>(sum); sum += dppf<0x4E>(sum); sum += dppf<0x141>(sum); sum += dppf<0x140>(sum);
            const float mean = sum * (1.0f / DM);
            float sq = 0.f;
#pragma unroll
            for (int i = 0; i < 40; ++i) { const float d = v[i] - mean; sq += d * d; }
            sq += dppf<0xB1>(sq); sq += dppf<0x4E>(sq); sq += dppf<0x141>(sq); sq += dppf<0x140>(sq);
            const float mb = rbf(mean), vb = rbf(sq * (1.0f / DM));
            const float rden = 1.0f / rbf(sqrtf(rbf(vb + 1e-5f)));
#pragma unroll
            for (int i = 0; i < 5; ++i) {
                v4u o;
#pragma unroll
                for (int j = 0; j < 4; ++j) o[j] = pack2(rbf(v[i * 8 + 2 * j] - mb) * rden, rbf(v[i * 8 + 2 * j + 1] - mb) * rden);
                const int c = li + 16 * i, kt = c >> 3, ch = c & 7;
                *reinterpret_cast<v4u*>(lds + AOFF + kt * 8192 + row * 128 + ((ch ^ (row & 7)) << 4)) = o;
            }
        }
    }
    f32x4 acc[2][6] = {};
    int cb = 0;
    for (int t = 0; t < 10; ++t) {
        if (t + NS - 1 <= 10) asm volatile("s_waitcnt vmcnt(%0)" ::"n"((NS - 2) * LPT) : "memory");
        else asm volatile("s_waitcnt vmcnt(0)" ::: "memory");
        asm volatile("s_waitcnt lgkmcnt(0)\n\ts_barrier" ::: "memory");
        if (t + NS - 1 < 10) stage_kn<BN, true>(B, FF, t + NS - 1, lds + (cb == 0 ? NS - 1 : cb - 1) * BB, tid, d2);
#pragma unroll
        for (int kk = 0; kk < 2; ++kk) {
            bf16x8 af[2], bfr[6];
#pragma unroll
            for (int m = 0; m < 2; ++m) af[m] = lds_frag(lds + AOFF + t * 8192, wr * 32 + m * 16 + fr, kk * 4 + fq);
#pragma unroll
            for (int n = 0; n < 6; ++n) bfr[n] = frag_kn<BN>(lds + cb * BB, (n & 1) * 6 + wc * 3 + (n >> 1), kk, lane);
#pragma unroll
            for (int m = 0; m < 2; ++m)
#pragma unroll
                for (int n = 0; n < 6; ++n) acc[m][n] = __builtin_amdgcn_mfma_f32_16x16x32_bf16(bfr[n], af[m], acc[m][n], 0, 0, 0);
        }
        cb = cb == NS - 1 ? 0 : cb + 1;
    }
    bf16_t* GU = (bf16_t*)(ws + OFF_GU);
#pragma unroll
    for (int m = 0; m < 2; ++m)
#pragma unroll
        for (int n = 0; n < 3; ++n) {
            float r[4];
#pragma unroll
            for (int j = 0; j < 4; ++j) r[j] = rbf(acc[m][2 * n][j]) * rbf(acc[m][2 * n + 1][j]);
            v2u o; o.x = pack2(r[0], r[1]); o.y = pack2(r[2], r[3]);
            *reinterpret_cast<v2u*>(GU + (size_t)(rb * 64 + wr * 32 + m * 16 + fr) * FF + bn * 96 + wc * 48 + n * 16 + fq * 4) = o;
        }
}

__global__ __launch_bounds__(256) void k_t_g6(unsigned char* ws, const bf16_t* __restrict__ Wd, bf16_t* __restrict__ out) {
    extern __shared__ __attribute__((aligned(16))) unsigned char lds[];
    constexpr int BM = 64, BN = 64;
    const int rb = FIN * 2 + blockIdx.x / 10, bn = blockIdx.x % 10;
    f32x4 acc[2][2] = {};
    gemm_mainloop_ms<BM, BN, true, 6>((const bf16_t*)(ws + OFF_GU) + (size_t)rb * BM * FF, FF, Wd + bn * BN, DM, FF, lds, acc);
    const bf16_t* H = (const bf16_t*)(ws + OFF_H);
    const int lane = threadIdx.x & 63, wid = threadIdx.x >> 6, wr = wid >> 1, wc = wid & 1, fr = lane & 15, fq = lane >> 4;
#pragma unroll
    for (int m = 0; m < 2; ++m)
#pragma unroll
        for (int n = 0; n < 2; ++n) {
            const size_t idx = (size_t)(rb * BM + wr * 32 + m * 16 + fr) * DM + bn * BN + wc * 32 + n * 16 + fq * 4;
            const v2u hv = *reinterpret_cast<const v2u*>(H + idx);
            v2u o;
            o.x = pack2(bf2f(hv.x & 0xffff) + rbf(acc[m][n][0]), bf2f(hv.x >> 16) + rbf(acc[m][n][1]));
            o.y = pack2(bf2f(hv.y & 0xffff) + rbf(acc[m][n][2]), bf2f(hv.y >> 16) + rbf(acc[m][n][3]));
            *reinterpret_cast<v2u*>(out + idx) = o;
        }
}

extern "C" void kernel_launch(void* const* d_in, const int* in_sizes, int n_in, void* d_out, int out_size, void* d_ws,
                              size_t ws_size, hipStream_t stream) {
    (void)in_sizes; (void)n_in; (void)out_size; (void)ws_size;
    const bf16_t* x = (const bf16_t*)d_in[0];
    const bf16_t* mem0 = (const bf16_t*)d_in[1];
    const bf16_t* Wq = (const bf16_t*)d_in[2];
    const bf16_t* Wk = (const bf16_t*)d_in[3];
    const bf16_t* Wv = (const bf16_t*)d_in[4];
    const bf16_t* Wo = (const bf16_t*)d_in[5];
    const bf16_t* Wg = (const bf16_t*)d_in[8];
    const bf16_t* Wu = (const bf16_t*)d_in[9];
    const bf16_t* Wd = (const bf16_t*)d_in[10];
    unsigned char* ws = (unsigned char*)d_ws;

    (void)hipFuncSetAttribute((const void*)k_pre, hipFuncAttributeMaxDynamicSharedMemorySize, 144 * 1024);
    (void)hipFuncSetAttribute((const void*)k_rec, hipFuncAttributeMaxDynamicSharedMemorySize, 160 * 1024);

    k_pre<<<256, 256, 144 * 1024, stream>>>(x, mem0, Wv, Wo, ws);
    k_rec<<<256, 256, 160 * 1024, stream>>>(Wq, Wk, Wv, Wo, Wg, Wu, Wd, x, (bf16_t*)d_out, ws);
    constexpr int NT = NFB - FIN;
    (void)hipFuncSetAttribute((const void*)k_t_g5, hipFuncAttributeMaxDynamicSharedMemorySize, 152 * 1024);
    (void)hipFuncSetAttribute((const void*)k_t_g6, hipFuncAttributeMaxDynamicSharedMemorySize, 96 * 1024);
    k_t_px<<<NT * 32, 256, 0, stream>>>(ws);
    k_t_g4<<<NT * 40, 256, 0, stream>>>(ws, x);
    k_t_g5<<<NT * 2 * 16, 256, 152 * 1024, stream>>>(ws, Wg, Wu);
    k_t_g6<<<NT * 2 * 10, 256, 96 * 1024, stream>>>(ws, Wd, (bf16_t*)d_out);
}
```
